```python
import torch

def _softmax(x, axis=-1):
    return torch.softmax(x, dim=axis)
def _gelu(x):
    return torch.nn.functional.gelu(x, approximate="tanh")
def _silu(x):
    return torch.nn.functional.silu(x)
def _sigmoid(x):
    return torch.sigmoid(x)
def _layernorm(x, axis=-1, eps=1e-5):
    m = x.mean(dim=axis, keepdim=True)
    v = x.var(dim=axis, keepdim=True, unbiased=False)
    return (x - m) / torch.sqrt(v + eps)
def _scatter_add(tgt, idx, src, axis=0):
    return tgt.clone().index_add_(axis, idx, src)
def _segment_sum(src, seg, n):
    out = torch.zeros((n,) + src.shape[1:], dtype=src.dtype)
    return out.index_add_(0, seg, src)
def _pad(x, pw):
    flat = tuple(v for pair in reversed(pw) for v in pair)
    return torch.nn.functional.pad(x, flat)

kernel_name = 'standalone__gqa_block_cached__sampling'

def setup_inputs():

    _w = (lambda a: (lambda t: t.to(torch.bfloat16) if t.is_floating_point() else t)(torch.as_tensor(a)))
    B, Sq, D = 32, 4, 1024
    Hq, Hkv, Sk, Dh = 32, 4, 2048, 128
    assert Hq % Hkv == 0, (Hq, Hkv)
    return {
        "x": _w((lambda *s: torch.randn(*s, dtype=torch.bfloat16) * 0.8)(B, Sq, D)),
        "k_cache": _w((lambda *s: torch.randn(*s, dtype=torch.bfloat16) * 0.8)(B, Hkv, Sk, Dh)),
        "v_cache": _w((lambda *s: torch.randn(*s, dtype=torch.bfloat16) * 0.05)(B, Hkv, Sk, Dh)),
        "wq": _w((lambda *s: torch.randn(*s, dtype=torch.bfloat16) * 0.8)(D, Hq * Dh) * (D ** -0.5)),
        "wo": _w((lambda *s: torch.randn(*s, dtype=torch.bfloat16) * 0.05)(Hq * Dh, D) * ((Hq * Dh) ** -0.5)),
    }

def reference(x, k_cache, v_cache, wq, wo):
    B, Sq, D = x.shape
    Hq, Dh = 32, 128
    Hkv = k_cache.shape[1]
    rep = Hq // Hkv
    q = (lambda x, *d: x.permute(*d))((x @ wq).reshape(B, Sq, Hq, Dh), 0, 2, 1, 3)
    k = (lambda x, n, axis: torch.repeat_interleave(x, n, dim=axis))(k_cache, rep, axis=1)
    v = (lambda x, n, axis: torch.repeat_interleave(x, n, dim=axis))(v_cache, rep, axis=1)
    scores = torch.einsum("bhqd,bhkd->bhqk", q, k) * (Dh ** -0.5)
    p = _softmax(scores, axis=-1)
    o = torch.einsum("bhqk,bhkd->bhqd", p, v)
    return (lambda x, *d: x.permute(*d))(o, 0, 2, 1, 3).reshape(B, Sq, Hq * Dh) @ wo
```

```cpp
#include <hip/hip_runtime.h>
#include <hip/hip_bf16.h>

#define GAS __attribute__((address_space(1)))
#define LAS __attribute__((address_space(3)))
typedef unsigned v4u __attribute__((ext_vector_type(4)));
typedef float f32x4 __attribute__((ext_vector_type(4)));
typedef short bf16x8 __attribute__((ext_vector_type(8)));
typedef __bf16 bf16x2 __attribute__((ext_vector_type(2)));
typedef float f32x2 __attribute__((ext_vector_type(2)));

#define KV_NT 0

__device__ __forceinline__ v4u ldnt(const GAS v4u* p) {
  return *p;
}
__device__ __forceinline__ f32x4 mfma(v4u a, v4u b, f32x4 c) {
  return __builtin_amdgcn_mfma_f32_16x16x32_bf16(__builtin_bit_cast(bf16x8, a), __builtin_bit_cast(bf16x8, b), c, 0, 0, 0);
}
__device__ __forceinline__ unsigned pk(float lo, float hi) {
  f32x2 v = {lo, hi};
  bf16x2 r = __builtin_convertvector(v, bf16x2);
  return __builtin_bit_cast(unsigned, r);
}
template <int I> __device__ __forceinline__ v4u trfrag(const v4u* v) {
  constexpr unsigned sel = (I & 1) ? 0x07060302u : 0x05040100u;
  v4u r;
  r.x = __builtin_amdgcn_perm(v[1][I >> 1], v[0][I >> 1], sel);
  r.y = __builtin_amdgcn_perm(v[3][I >> 1], v[2][I >> 1], sel);
  r.z = __builtin_amdgcn_perm(v[5][I >> 1], v[4][I >> 1], sel);
  r.w = __builtin_amdgcn_perm(v[7][I >> 1], v[6][I >> 1], sel);
  return r;
}

__global__ void __launch_bounds__(512) k_qproj(const unsigned short* x, const unsigned short* wq, unsigned short* q) {
  __shared__ f32x4 red[8][8][64];
  const int lane = threadIdx.x & 63, w = __builtin_amdgcn_readfirstlane(threadIdx.x >> 6);
  const int c = lane & 15, g = lane >> 4;
  const int nt = blockIdx.x & 31, mt = blockIdx.x >> 5;
  f32x4 acc[8];
#pragma unroll
  for (int i = 0; i < 8; ++i) acc[i] = f32x4{0, 0, 0, 0};
#pragma unroll
  for (int kb = 0; kb < 4; ++kb) {
    const int k0 = 128 * w + 32 * kb;
    v4u wv[8];
#pragma unroll
    for (int j = 0; j < 8; ++j) wv[j] = *(const GAS v4u*)(wq + (size_t)(k0 + 8 * g + j) * 4096 + nt * 128 + 8 * c);
    v4u xf = *(const GAS v4u*)(x + (size_t)(mt * 16 + c) * 1024 + k0 + 8 * g);
    acc[0] = mfma(trfrag<0>(wv), xf, acc[0]); acc[1] = mfma(trfrag<1>(wv), xf, acc[1]);
    acc[2] = mfma(trfrag<2>(wv), xf, acc[2]); acc[3] = mfma(trfrag<3>(wv), xf, acc[3]);
    acc[4] = mfma(trfrag<4>(wv), xf, acc[4]); acc[5] = mfma(trfrag<5>(wv), xf, acc[5]);
    acc[6] = mfma(trfrag<6>(wv), xf, acc[6]); acc[7] = mfma(trfrag<7>(wv), xf, acc[7]);
  }
#pragma unroll
  for (int i = 0; i < 8; ++i) red[w][i][lane] = acc[i];
  __syncthreads();
  f32x4 s = red[0][w][lane];
#pragma unroll
  for (int ww = 1; ww < 8; ++ww) s += red[ww][w][lane];
#pragma unroll
  for (int r = 0; r < 4; ++r) q[(size_t)(mt * 16 + c) * 4096 + nt * 128 + 32 * g + 8 * r + w] = (unsigned short)(pk(s[r], 0.f) & 0xffffu);
}

constexpr int WIN = 24;
__global__ void __launch_bounds__(512, 2) k_attn(const unsigned short* q, const unsigned short* kc, const unsigned short* vc, float* po, float* pml) {
  extern __shared__ __attribute__((aligned(16))) unsigned char smem[];
  LAS f32x4* red = (LAS f32x4*)smem;
  LAS float* mx = (LAS float*)(smem + 131072);
  LAS float* ls = (LAS float*)(smem + 131072 + 1024);
  const int lane = threadIdx.x & 63, w = __builtin_amdgcn_readfirstlane(threadIdx.x >> 6);
  const int c = lane & 15, g = lane >> 4;
  const int pair = blockIdx.x >> 1, half = blockIdx.x & 1, b = pair >> 2, kvh = pair & 3;
  const size_t kvbase = ((size_t)pair * 2048 + half * 1024 + w * 128) * 128;
  const GAS unsigned char* kp = (const GAS unsigned char*)(kc + kvbase) + (4 * (c & 3) + (c >> 2)) * 256 + 16 * g;
  const GAS unsigned char* vp = (const GAS unsigned char*)(vc + kvbase) + lane * 16;
  v4u qf[2][4];
#pragma unroll
  for (int nt = 0; nt < 2; ++nt) {
    const int R = c + 16 * nt;
    const GAS unsigned short* qr = (const GAS unsigned short*)q + (size_t)(b * 4 + (R & 3)) * 4096 + (kvh * 8 + (R >> 2)) * 128 + 8 * g;
#pragma unroll
    for (int s = 0; s < 4; ++s) qf[nt][s] = *(const GAS v4u*)(qr + 32 * s);
  }
  auto addr = [&](int i) -> const GAS v4u* {
    if (i < 32) return (const GAS v4u*)(kp + (i >> 2) * 4096 + (i & 3) * 64);
    return (const GAS v4u*)(vp + (i - 32) * 1024);
  };
  v4u a[WIN];
#pragma unroll
  for (int i = 0; i < WIN; ++i) a[i] = ldnt(addr(i));
  __builtin_amdgcn_sched_barrier(0);
  f32x4 S[8][2];
#pragma unroll
  for (int t = 0; t < 8; ++t) { S[t][0] = f32x4{0, 0, 0, 0}; S[t][1] = f32x4{0, 0, 0, 0}; }
#pragma unroll
  for (int i = 0; i < 32; ++i) {
    const v4u cur = a[i % WIN];
    S[i >> 2][0] = mfma(cur, qf[0][i & 3], S[i >> 2][0]);
    S[i >> 2][1] = mfma(cur, qf[1][i & 3], S[i >> 2][1]);
    __builtin_amdgcn_sched_barrier(0);
    a[i % WIN] = ldnt(addr(i + WIN));
    __builtin_amdgcn_sched_barrier(0);
  }
  const float C = 0.088388347648318440f * 1.4426950408889634f;
  float m[2];
#pragma unroll
  for (int nt = 0; nt < 2; ++nt) {
    float v = S[0][nt][0];
#pragma unroll
    for (int t = 0; t < 8; ++t)
#pragma unroll
      for (int r = 0; r < 4; ++r) v = fmaxf(v, S[t][nt][r]);
    v = fmaxf(v, __shfl_xor(v, 16));
    v = fmaxf(v, __shfl_xor(v, 32));
    m[nt] = v;
  }
  if (g == 0) { mx[w * 32 + c] = m[0]; mx[w * 32 + 16 + c] = m[1]; }
  asm volatile("s_waitcnt lgkmcnt(0)" ::: "memory");
  __builtin_amdgcn_s_barrier();
  asm volatile("" ::: "memory");
#pragma unroll
  for (int nt = 0; nt < 2; ++nt) {
    float v = mx[16 * nt + c];
#pragma unroll
    for (int ww = 1; ww < 8; ++ww) v = fmaxf(v, mx[ww * 32 + 16 * nt + c]);
    m[nt] = v;
  }
  v4u P[4][2];
  float l[2];
#pragma unroll
  for (int nt = 0; nt < 2; ++nt) {
    const float mc = m[nt] * C;
    float sum = 0.f;
#pragma unroll
    for (int kb = 0; kb < 4; ++kb) {
      float p[8];
#pragma unroll
      for (int j = 0; j < 8; ++j) { p[j] = __builtin_amdgcn_exp2f(fmaf(S[2 * kb + (j >> 2)][nt][j & 3], C, -mc)); sum += p[j]; }
      P[kb][nt] = v4u{pk(p[0], p[1]), pk(p[2], p[3]), pk(p[4], p[5]), pk(p[6], p[7])};
    }
    sum += __shfl_xor(sum, 16);
    sum += __shfl_xor(sum, 32);
    l[nt] = sum;
  }
  if (g == 0) { ls[w * 32 + c] = l[0]; ls[w * 32 + 16 + c] = l[1]; }
  f32x4 O[8][2];
#pragma unroll
  for (int i = 0; i < 8; ++i) { O[i][0] = f32x4{0, 0, 0, 0}; O[i][1] = f32x4{0, 0, 0, 0}; }
  __builtin_amdgcn_sched_barrier(0);
#pragma unroll
  for (int kb = 0; kb < 4; ++kb) {
    v4u v[8];
#pragma unroll
    for (int j = 0; j < 8; ++j) v[j] = a[(32 + 8 * kb + j) % WIN];
#define PV(I) { const v4u f = trfrag<I>(v); O[I][0] = mfma(f, P[kb][0], O[I][0]); O[I][1] = mfma(f, P[kb][1], O[I][1]); }
    PV(0) PV(1) PV(2) PV(3) PV(4) PV(5) PV(6) PV(7)
#undef PV
    __builtin_amdgcn_sched_barrier(0);
#pragma unroll
    for (int j = 0; j < 8; ++j) { const int i = 32 + 8 * kb + j; if (i + WIN < 64) a[i % WIN] = ldnt(addr(i + WIN)); }
    __builtin_amdgcn_sched_barrier(0);
  }
#pragma unroll
  for (int i = 0; i < 8; ++i) { red[(w * 16 + i * 2 + 0) * 64 + lane] = O[i][0]; red[(w * 16 + i * 2 + 1) * 64 + lane] = O[i][1]; }
  __syncthreads();
#pragma unroll
  for (int nt = 0; nt < 2; ++nt) {
    f32x4 s = red[(0 * 16 + w * 2 + nt) * 64 + lane];
#pragma unroll
    for (int ww = 1; ww < 8; ++ww) s += red[(ww * 16 + w * 2 + nt) * 64 + lane];
#pragma unroll
    for (int r = 0; r < 4; ++r) po[((size_t)blockIdx.x * 32 + c + 16 * nt) * 128 + 32 * g + 8 * r + w] = s[r];
  }
  if (threadIdx.x < 32) {
    float M = mx[threadIdx.x], L = ls[threadIdx.x];
    for (int ww = 1; ww < 8; ++ww) { M = fmaxf(M, mx[ww * 32 + threadIdx.x]); L += ls[ww * 32 + threadIdx.x]; }
    pml[(blockIdx.x * 32 + threadIdx.x) * 2] = M * C;
    pml[(blockIdx.x * 32 + threadIdx.x) * 2 + 1] = L;
  }
}

__global__ void __launch_bounds__(1024) k_oproj(const float* po, const float* pml, const unsigned short* wo, unsigned short* out) {
  extern __shared__ __attribute__((aligned(16))) unsigned char smem[];
  LAS f32x4* red = (LAS f32x4*)smem;
  const int lane = threadIdx.x & 63, w = __builtin_amdgcn_readfirstlane(threadIdx.x >> 6);
  const int c = lane & 15, g = lane >> 4;
  const int nt = blockIdx.x & 7, mt = blockIdx.x >> 3;
  const int tok = mt * 16 + c, b = tok >> 2, pos = tok & 3;
  f32x4 acc[8];
#pragma unroll
  for (int i = 0; i < 8; ++i) acc[i] = f32x4{0, 0, 0, 0};
#pragma unroll 2
  for (int kb = 0; kb < 8; ++kb) {
    const int k0 = 256 * w + 32 * kb;
    v4u wv[8];
#pragma unroll
    for (int j = 0; j < 8; ++j) wv[j] = *(const GAS v4u*)(wo + (size_t)(k0 + 8 * g + j) * 1024 + nt * 128 + 8 * c);
    const int hq = k0 >> 7, kvh = hq >> 3, R = (hq & 7) * 4 + pos, d0 = (k0 & 127) + 8 * g;
    const int wg0 = (b * 4 + kvh) * 2;
    const float m0 = pml[(wg0 * 32 + R) * 2], l0 = pml[(wg0 * 32 + R) * 2 + 1];
    const float m1 = pml[((wg0 + 1) * 32 + R) * 2], l1 = pml[((wg0 + 1) * 32 + R) * 2 + 1];
    const float M = fmaxf(m0, m1), e0 = __builtin_amdgcn_exp2f(m0 - M), e1 = __builtin_amdgcn_exp2f(m1 - M);
    const float inv = 1.f / (e0 * l0 + e1 * l1), w0 = e0 * inv, w1 = e1 * inv;
    const GAS f32x4* p0 = (const GAS f32x4*)(po + ((size_t)wg0 * 32 + R) * 128 + d0);
    const GAS f32x4* p1 = (const GAS f32x4*)(po + ((size_t)(wg0 + 1) * 32 + R) * 128 + d0);
    const f32x4 a0 = p0[0] * w0 + p1[0] * w1, a1 = p0[1] * w0 + p1[1] * w1;
    const v4u xf = v4u{pk(a0[0], a0[1]), pk(a0[2], a0[3]), pk(a1[0], a1[1]), pk(a1[2], a1[3])};
    acc[0] = mfma(trfrag<0>(wv), xf, acc[0]); acc[1] = mfma(trfrag<1>(wv), xf, acc[1]);
    acc[2] = mfma(trfrag<2>(wv), xf, acc[2]); acc[3] = mfma(trfrag<3>(wv), xf, acc[3]);
    acc[4] = mfma(trfrag<4>(wv), xf, acc[4]); acc[5] = mfma(trfrag<5>(wv), xf, acc[5]);
    acc[6] = mfma(trfrag<6>(wv), xf, acc[6]); acc[7] = mfma(trfrag<7>(wv), xf, acc[7]);
  }
#pragma unroll
  for (int i = 0; i < 8; ++i) red[(w * 8 + i) * 64 + lane] = acc[i];
  __syncthreads();
  if (w < 8) {
    f32x4 s = red[(0 * 8 + w) * 64 + lane];
#pragma unroll
    for (int ww = 1; ww < 16; ++ww) s += red[(ww * 8 + w) * 64 + lane];
#pragma unroll
    for (int r = 0; r < 4; ++r) out[(size_t)tok * 1024 + nt * 128 + 32 * g + 8 * r + w] = (unsigned short)(pk(s[r], 0.f) & 0xffffu);
  }
}

extern "C" void kernel_launch(void* const* d_in, const int* in_sizes, int n_in, void* d_out, int out_size, void* d_ws, size_t ws_size,
                              hipStream_t stream) {
  const unsigned short* x = (const unsigned short*)d_in[0];
  const unsigned short* kc = (const unsigned short*)d_in[1];
  const unsigned short* vc = (const unsigned short*)d_in[2];
  const unsigned short* wq = (const unsigned short*)d_in[3];
  const unsigned short* wo = (const unsigned short*)d_in[4];
  unsigned char* ws = (unsigned char*)d_ws;
  unsigned short* q = (unsigned short*)ws;
  float* po = (float*)(ws + (1 << 20));
  float* pml = (float*)(ws + (6 << 20));
  (void)hipFuncSetAttribute((const void*)k_attn, hipFuncAttributeMaxDynamicSharedMemorySize, 131072 + 2048);
  (void)hipFuncSetAttribute((const void*)k_oproj, hipFuncAttributeMaxDynamicSharedMemorySize, 131072);
  k_qproj<<<256, 512, 0, stream>>>(x, wq, q);
  k_attn<<<256, 512, 131072 + 2048, stream>>>(q, kc, vc, po, pml);
  k_oproj<<<64, 1024, 131072, stream>>>(po, pml, wo, (unsigned short*)d_out);
}
```

```cpp
#include <hip/hip_runtime.h>
#include <hip/hip_bf16.h>

#define LAS __attribute__((address_space(3)))
typedef unsigned v4u __attribute__((ext_vector_type(4)));
typedef unsigned v2u __attribute__((ext_vector_type(2)));
typedef float f32x4 __attribute__((ext_vector_type(4)));
typedef float f32x2 __attribute__((ext_vector_type(2)));
typedef short bf16x8 __attribute__((ext_vector_type(8)));
typedef short s16x4 __attribute__((ext_vector_type(4)));
typedef unsigned short u16x2 __attribute__((ext_vector_type(2)));
typedef __bf16 bf16x2 __attribute__((ext_vector_type(2)));
typedef __amdgpu_buffer_rsrc_t rsrc_t;

constexpr int LDS_MISC = 90112, LDS_MX = LDS_MISC, LDS_CHK = LDS_MISC + 4096;
constexpr int LDS_P = 98304, LDS_QF = 57344, LDS_BYTES = 163840;
constexpr unsigned KEEPER = 58;
constexpr unsigned WS_T = 1024, WS_F = 4096, WS_S = 1u << 17, WS_Q = 1u << 20, WS_X = 2u << 20, WS_O = 3u << 20, WS_P = 4u << 20, WS_BYTES = 6u << 20;
constexpr unsigned OOB = 0x80000000u;
constexpr int SC1 = 16;
constexpr unsigned SPIN_CAP = 1u << 16;
constexpr int DELAY1 = 0;
constexpr int DELAY2 = 8;
constexpr int MSPLIT = 18;
constexpr int MLEN = 10;
constexpr int MV = 3;
constexpr int LEAD = 20;
constexpr int TNOM = 188;
constexpr unsigned HM = 0x5;
constexpr int WALT = 0;
constexpr int AROT = 1;
constexpr int KB0 = 1;
constexpr int KB1 = 3;
constexpr int KB2 = 4;
constexpr int KB3 = 6;
constexpr int KB4 = 8;
constexpr int KB5 = 2;
constexpr int POLL1 = 1;
constexpr int POLL2 = 1;
constexpr float SM_C = 0.088388347648318440f * 1.4426950408889634f;

__device__ __forceinline__ f32x4 mfma(v4u a, v4u b, f32x4 c) {
  return __builtin_amdgcn_mfma_f32_16x16x32_bf16(__builtin_bit_cast(bf16x8, a), __builtin_bit_cast(bf16x8, b), c, 0, 0, 0);
}
__device__ __forceinline__ unsigned pk(float lo, float hi) {
  const f32x2 v = {lo, hi};
  return __builtin_bit_cast(unsigned, __builtin_convertvector(v, bf16x2));
}
__device__ __forceinline__ v4u lds128(unsigned a) { return *(const LAS v4u*)(size_t)a; }
__device__ __forceinline__ v4u tr_frag(unsigned a0, unsigned a1) {
  const v2u lo = __builtin_bit_cast(v2u, __builtin_amdgcn_ds_read_tr16_b64_v4i16((LAS s16x4*)(size_t)a0));
  const v2u hi = __builtin_bit_cast(v2u, __builtin_amdgcn_ds_read_tr16_b64_v4i16((LAS s16x4*)(size_t)a1));
  return v4u{lo.x, lo.y, hi.x, hi.y};
}
__device__ __forceinline__ float mx2(float a, float b) { return __builtin_elementwise_maximum(a, b); }
constexpr unsigned LOWN = 0x000F000Fu;
__device__ __forceinline__ unsigned low_nibbles(unsigned t) { return (t & 0xFu) | ((t & 0xF0u) << 12); }
__device__ __forceinline__ v2u enc_ps(f32x4 v, unsigned tp) {
  const float m = mx2(mx2(mx2(__builtin_fabsf(v[0]), __builtin_fabsf(v[1])), __builtin_fabsf(v[2])), __builtin_fabsf(v[3]));
  const unsigned e = (__float_as_uint(m) >> 23) & 0xFFu;
  const int k = 126 - (int)e;
  const unsigned a = __builtin_bit_cast(unsigned, __builtin_amdgcn_cvt_pknorm_i16(__builtin_amdgcn_ldexpf(v[0], k), __builtin_amdgcn_ldexpf(v[1], k)));
  const unsigned b = __builtin_bit_cast(unsigned, __builtin_amdgcn_cvt_pknorm_i16(__builtin_amdgcn_ldexpf(v[2], k), __builtin_amdgcn_ldexpf(v[3], k)));
  return v2u{(a & ~LOWN) | low_nibbles(e), (b & ~LOWN) | tp};
}
__device__ __forceinline__ f32x4 dec_ps(unsigned x0, unsigned x1) {
  const unsigned e = (x0 & 0xFu) | ((x0 >> 12) & 0xF0u);
  const float sc = e == 255u ? __uint_as_float(0x7FC00000u) : __builtin_amdgcn_ldexpf(1.0f / 32767.0f, (int)e - 126);
  const unsigned a = (x0 & ~LOWN) | 0x00080008u, b = (x1 & ~LOWN) | 0x00080008u;
  return f32x4{(float)(short)a * sc, (float)((int)a >> 16) * sc, (float)(short)b * sc, (float)((int)b >> 16) * sc};
}
constexpr unsigned SPARE = 0x78007800u;
__device__ __forceinline__ unsigned spread_byte(unsigned t) { return ((t & 0xF0u) << 7) | ((t & 0xFu) << 27); }
__device__ __forceinline__ v2u enc_bf(f32x4 v, unsigned tp) {
  const unsigned a = pk(v[0], v[1]), b = pk(v[2], v[3]);
  const u16x2 ua = __builtin_bit_cast(u16x2, a & 0x7FFF7FFFu), ub = __builtin_bit_cast(u16x2, b & 0x7FFF7FFFu);
  const u16x2 m2 = __builtin_elementwise_max(ua, ub);
  const unsigned base = (unsigned)max((int)(max((unsigned)m2.x, (unsigned)m2.y) & 0x7F80u) - 0x780, 0);
  const u16x2 b2 = {(unsigned short)base, (unsigned short)base};
  const unsigned ta = __builtin_bit_cast(unsigned, __builtin_elementwise_sub_sat(ua, b2));
  const unsigned tb = __builtin_bit_cast(unsigned, __builtin_elementwise_sub_sat(ub, b2));
  return v2u{ta | (a & 0x80008000u) | (base & 0x7800u) | ((base & 0x780u) << 20), tb | (b & 0x80008000u) | tp};
}
__device__ __forceinline__ v2u dec_bf(unsigned x0, unsigned x1) {
  const unsigned base = (x0 & 0x7800u) | ((x0 >> 20) & 0x780u), b2 = base | (base << 16);
  return v2u{(x0 & ~SPARE) + b2, (x1 & ~SPARE) + b2};
}
__device__ __forceinline__ v4u dec_bf2(v4u u) {
  const v2u a = dec_bf(u.x, u.y), b = dec_bf(u.z, u.w);
  return v4u{a.x, a.y, b.x, b.y};
}
__device__ __forceinline__ v2u enc_fx(f32x4 v, unsigned tp) {
  const v4u u = __builtin_bit_cast(v4u, v), a = u & 0x7FFFFFFFu;
  const unsigned e = max(max(a.x, a.y), max(a.z, a.w)) >> 23;
  unsigned t[4];
#pragma unroll
  for (int i = 0; i < 4; ++i)
    t[i] = min((unsigned)__builtin_rintf(__builtin_amdgcn_ldexpf(__uint_as_float(min(a[i], 0x7F7FFFFFu)), 137 - (int)e)), 2047u) | ((u[i] >> 16) & 0x8000u);
  return v2u{t[0] | (t[1] << 16) | spread_byte(e), t[2] | (t[3] << 16) | tp};
}
__device__ __forceinline__ f32x4 dec_fx(v2u x) {
  const unsigned e = ((x.x >> 7) & 0xF0u) | ((x.x >> 27) & 0xFu);
  f32x4 r;
#pragma unroll
  for (int i = 0; i < 4; ++i) {
    const unsigned f = (i & 1) ? x[i >> 1] >> 16 : x[i >> 1];
    const unsigned m = __float_as_uint(__builtin_amdgcn_ldexpf((float)(f & 0x7FFu), (int)e - 137));
    r[i] = __uint_as_float(e == 255u ? 0x7FC00000u : (m | ((f & 0x8000u) << 16)));
  }
  return r;
}
__device__ __forceinline__ v4u pair_rows(v2u x) {
  const auto a = __builtin_amdgcn_permlane16_swap(x.x, x.x, false, false), b = __builtin_amdgcn_permlane16_swap(x.y, x.y, false, false);
  return v4u{a[0], b[0], a[1], b[1]};
}
__device__ __forceinline__ unsigned key_of(unsigned p) {
  return ((p & 1) << KB0) | (((p >> 1) & 1) << KB1) | (((p >> 2) & 1) << KB2) | (((p >> 3) & 1) << KB3) | (((p >> 4) & 1) << KB4) | (((p >> 5) & 1) << KB5);
}
__device__ __forceinline__ unsigned sel(bool p, unsigned a, unsigned b) { const unsigned m = 0u - (unsigned)p; return (a & m) | (b & ~m); }
__device__ __forceinline__ unsigned xor_or(unsigned a, unsigned b, unsigned d) { return __builtin_amdgcn_bitop3_b32(a, b, d, 0xBE); }
__device__ __forceinline__ unsigned add_bytes(unsigned a, unsigned b) {
  return ((a & 0x7F7F7F7Fu) + (b & 0x7F7F7F7Fu)) ^ ((a ^ b) & 0x80808080u);
}
__device__ __forceinline__ v4u ldg(rsrc_t rs, unsigned off) { return __builtin_amdgcn_raw_buffer_load_b128(rs, off, 0, SC1); }
__device__ __forceinline__ void stg(v4u v, rsrc_t rs, unsigned off) { __builtin_amdgcn_raw_buffer_store_b128(v, rs, off, 0, SC1); }
__device__ __forceinline__ bool any_bad(unsigned d) { return __builtin_amdgcn_ballot_w64(d != 0u) != 0ull; }

#define PIN(X) asm volatile("" : "+v"(X))
#define MEMFENCE() asm volatile("" ::: "memory")
#define FENCE() __builtin_amdgcn_sched_barrier(0)
#define SYNC(N) asm volatile("s_waitcnt vmcnt(" #N ") lgkmcnt(0)\n\ts_barrier" ::: "memory")
#define SYNC_P(N, DUE)                                                                                                      \
  asm volatile("s_cmp_lg_u32 %1, 0\n\ts_cbranch_scc1 .Lq%=\n.Lp%=:\n\ts_memrealtime vcc\n\t"                                \
               "s_waitcnt vmcnt(" #N ") lgkmcnt(0)\n\ts_sub_u32 vcc_lo, vcc_lo, %0\n\t"                                     \
               "s_cmpk_lt_u32 vcc_lo, " #DUE "\n\ts_cbranch_scc1 .Lp%=\n.Lq%=:\n\t"                                         \
               "s_waitcnt vmcnt(" #N ") lgkmcnt(0)\n\ts_barrier"                                                            \
               :: "s"(tbase), "s"(w) : "memory", "scc", "vcc")
#define DMA_AT(OP, POL, RS, VOFF, SOFF, BASE, LDS)                                                                          \
  do {                                                                                                                      \
    unsigned t_;                                                                                                            \
    asm volatile("s_add_u32 m0, %1, %3\n\ts_mov_b32 %0, %4\n\t" OP " %2, %5, %0 offen " POL " lds"                          \
                 : "=&s"(t_) : "s"(BASE), "v"(VOFF), "i"(LDS), "i"(SOFF), "s"(RS) : "memory", "scc");                       \
  } while (0)
#define DMA(POL, RS, VOFF, SOFF, LDS) DMA_AT("buffer_load_dwordx4", POL, RS, VOFF, SOFF, ldsw, LDS)
#define DMA_R(POL, RS, VOFF, ROT, SOFF, MASK, LDS)                                                                          \
  do {                                                                                                                      \
    unsigned t_;                                                                                                            \
    asm volatile("s_add_u32 m0, %1, %3\n\ts_add_u32 %0, %6, %4\n\ts_and_b32 %0, %0, %7\n\t"                                 \
                 "buffer_load_dwordx4 %2, %5, %0 offen " POL " lds"                                                         \
                 : "=&s"(t_) : "s"(ldsw), "v"(VOFF), "i"(LDS), "i"(SOFF), "s"(RS), "s"(ROT), "i"(MASK) : "memory", "scc");  \
  } while (0)
#define DMA_X(POL, RS, VOFF, XOR, SOFF, LDS)                                                                                \
  do {                                                                                                                      \
    unsigned t_;                                                                                                            \
    asm volatile("s_add_u32 m0, %1, %3\n\ts_xor_b32 %0, %6, %4\n\tbuffer_load_dwordx4 %2, %5, %0 offen " POL " lds"         \
                 : "=&s"(t_) : "s"(ldsw), "v"(VOFF), "i"(LDS), "i"(SOFF), "s"(RS), "s"(XOR) : "memory", "scc");             \
  } while (0)

#define SMALL_ISSUE()                                                                                                       \
  {                                                                                                                         \
    unsigned long long pc_;                                                                                                 \
    unsigned len_;                                                                                                          \
    asm volatile("s_getpc_b64 %0\n.Lgqa_pc:\n\ts_mov_b32 %1, .Lgqa_end-.Lgqa_pc" : "=s"(pc_), "=s"(len_));                  \
    const rsrc_t rc_ = __builtin_amdgcn_make_buffer_rsrc(w ? (void*)pc_ : epochp, 0, w ? len_ : 4u, 0x00020000);            \
    const unsigned o_ = (w ? min((((w - 1) + 7 * lane) * 32 + r) * 128, len_ - 4) : 0u) | (lane < 2 ? 0u : OOB);            \
    DMA_AT("buffer_load_dword", "sc1", rc_, o_, 0, ldsq, LDS_CHK);                                                          \
  }
#define READ_EPOCH()                                                                                                        \
  tag32 = add_bytes(__builtin_amdgcn_readfirstlane(*(const LAS unsigned*)(size_t)(lds0 + LDS_CHK)), 0xA76B2F5Du);           \
  tg = v4u{tag32 & 0xFFu, (tag32 >> 8) & 0xFFu, (tag32 >> 16) & 0xFFu, tag32 >> 24};                                         \
  tA = spread_byte((c & 1) ? tg.z : tg.x);                                                                                  \
  tB = spread_byte((c & 1) ? tg.w : tg.y);                                                                                  \
  PIN(tA); PIN(tB)
#define T_MARK() tfirst = (unsigned)__builtin_amdgcn_s_memrealtime()
#define T_PUBLISH() if (r < 8 && w == 1) stg(v4u{tfirst, tag32, tfirst, ~tag32}, rsWs, lane == 0 ? WS_T + (xcd * 2 + (r >> 2)) * 16 : OOB)
#define T_ISSUE() { const unsigned o_ = lane < 8 ? WS_T + w * 32 + lane * 4 : OOB; DMA_AT("buffer_load_dword", "sc1", rsWs, o_, 0, ldsq, LDS_CHK); }
#define T_READ()                                                                                                            \
  if (w == 0) {                                                                                                             \
    const v4u u_ = lds128(lds0 + LDS_CHK + (c >> 1) * 256 + (c & 1) * 16);                                                                                    \
    const bool ok_ = lane < 16 && u_.y == tag32 && u_.w == ~tag32 && u_.x - tstart - TNOM + 300u <= 800u;                          \
    const int d_ = ok_ ? (int)(u_.x - tstart) - TNOM : 0;                                                                   \
    int s_ = 0;                                                                                                             \
    _Pragma("unroll") for (int i_ = 0; i_ < 16; ++i_) s_ += __builtin_amdgcn_readlane(d_, i_);                              \
    tbase += s_ >> 4;                                                                                                       \
  }
#define T_LEAD() tbase -= burst ? LEAD : 0
#define RD_X(N, S) const v4u x0_##N = lds128(aX[0] + (S)), x1_##N = lds128(aX[1] + (S)), x2_##N = lds128(aX[2] + (S)), x3_##N = lds128(aX[3] + (S))
#define RD_W(N, S) const v4u w0_##N = tr_frag(aN + (S), aN + (S) + 2048), w1_##N = tr_frag(aN + (S) + 4096, aN + (S) + 6144)
#define MM_W(N, XA, XB) qa = mfma(w0_##N, XA, qa); qa = mfma(w1_##N, XB, qa); PIN(qa)
#define PUBLISH_Q()                                                                                                         \
  {                                                                                                                         \
    const v4u x_ = pair_rows(enc_bf(qa, (g & 1) ? tB : tA));                                                                \
    stg(v4u{x_.x, (g & 1) ? tag32 : x_.y, x_.z, (g & 1) ? ~tag32 : x_.w}, rsWs, offQp);                                     \
  }
#define K_INIT(J) _Pragma("unroll") for (int f_ = 0; f_ < 8; ++f_) asm volatile("" : "=v"(kf[J][f_]))
#define K_HI(S) ((S) >= 57344 ? 32768 : 0)
#define CONS_K(S, J, W)                                                                                                     \
  asm volatile("s_cmp_lg_u32 %12, %15\n\ts_cbranch_scc1 .Lk%=\n\t"                                                          \
               "ds_read_b128 %0, %8 offset:%13\n\tds_read_b128 %1, %9 offset:%13\n\t"                                       \
               "ds_read_b128 %2, %10 offset:%13\n\tds_read_b128 %3, %11 offset:%13\n\t"                                     \
               "ds_read_b128 %4, %8 offset:%14\n\tds_read_b128 %5, %9 offset:%14\n\t"                                       \
               "ds_read_b128 %6, %10 offset:%14\n\tds_read_b128 %7, %11 offset:%14\n\t"                                     \
               "s_waitcnt lgkmcnt(0)\n.Lk%=:"                                                                               \
               : "+v"(kf[J][0]), "+v"(kf[J][1]), "+v"(kf[J][2]), "+v"(kf[J][3]), "+v"(kf[J][4]), "+v"(kf[J][5]),            \
                 "+v"(kf[J][6]), "+v"(kf[J][7])                                                                             \
               : "v"(aK[0] + K_HI(S)), "v"(aK[1] + K_HI(S)), "v"(aK[2] + K_HI(S)), "v"(aK[3] + K_HI(S)), "s"(w),            \
                 "i"((S) - K_HI(S)), "i"((S) - K_HI(S) + 4096), "i"(W)                                                      \
               : "memory", "scc")
#define CONS_G(S)                                                                                                           \
  {                                                                                                                         \
    v4u g_ = lds128(aLw + (S));                                                                                             \
    if (__builtin_expect(any_bad(xor_or(g_.y, tA, g_.w ^ tB) & SPARE), 0)) {                                                \
      _Pragma("unroll 1") for (unsigned sp_ = 0; sp_ < SPIN_CAP; ++sp_) {                                                   \
        MEMFENCE();                                                                                                         \
        g_ = ldg(rsWs, voffQ);                                                                                              \
                                                                                                                  \
        if (!any_bad(xor_or(g_.y, tA, g_.w ^ tB) & SPARE)) break;                                                           \
        __builtin_amdgcn_s_sleep(8);                                                                                        \
      }                                                                                                                     \
    }                                                                                                                       \
    *(LAS v4u*)(size_t)(aP + LDS_QF + w * 1024) = dec_bf2(g_);                                                              \
  }
#define Q_READ() _Pragma("unroll") for (int f_ = 0; f_ < 8; ++f_) qf[f_ >> 2][f_ & 3] = lds128(aP + LDS_QF + f_ * 1024)
#define SM_S(J, NT)                                                                                                         \
  _Pragma("unroll") for (int t_ = 0; t_ < 2; ++t_) {                                                                        \
    f32x4 z_ = {0.f, 0.f, 0.f, 0.f};                                                                                        \
    _Pragma("unroll") for (int s_ = 0; s_ < 4; ++s_) z_ = mfma(kf[J][4 * t_ + s_], qf[NT][s_], z_);                         \
    sc[(J) & 1][t_][NT] = z_;                                                                                               \
  }
#define SM_PIN(J, NT) PIN(sc[(J) & 1][0][NT]); PIN(sc[(J) & 1][1][NT])
#define SM_M(J)                                                                                                             \
  {                                                                                                                         \
    float m_[2];                                                                                                            \
    _Pragma("unroll") for (int n_ = 0; n_ < 2; ++n_) {                                                                      \
      const f32x4 a_ = sc[(J) & 1][0][n_], b_ = sc[(J) & 1][1][n_];                                                         \
      m_[n_] = mx2(mx2(mx2(mx2(a_[0], a_[1]), a_[2]), mx2(mx2(a_[3], b_[0]), b_[1])), mx2(b_[2], b_[3]));                   \
    }                                                                                                                       \
    auto s_ = __builtin_amdgcn_permlane16_swap(__float_as_uint(m_[0]), __float_as_uint(m_[1]), false, false);               \
    const float y_ = mx2(__uint_as_float(s_[0]), __uint_as_float(s_[1]));                                                   \
    auto t_ = __builtin_amdgcn_permlane32_swap(__float_as_uint(y_), __float_as_uint(y_), false, false);                     \
    *(LAS float*)(size_t)(aRw + ((J) & 1) * 1024) = mx2(__uint_as_float(t_[0]), __uint_as_float(t_[1]));                    \
  }
#define RD_M(J)                                                                                                             \
  const f32x4 mu0_##J = __builtin_bit_cast(f32x4, lds128(aRr + ((J) & 1) * 1024)),                                          \
              mv0_##J = __builtin_bit_cast(f32x4, lds128(aRr + ((J) & 1) * 1024 + 16)),                                     \
              mu1_##J = __builtin_bit_cast(f32x4, lds128(aRr + ((J) & 1) * 1024 + 512)),                                    \
              mv1_##J = __builtin_bit_cast(f32x4, lds128(aRr + ((J) & 1) * 1024 + 528))
#define SM_B(J, NT)                                                                                                         \
  {                                                                                                                         \
    const f32x4 u_ = mu##NT##_##J, v_ = mv##NT##_##J;                                                                       \
    float mn_ = mx2(mx2(mx2(mx2(u_[0], u_[1]), u_[2]), mx2(mx2(u_[3], v_[0]), v_[1])), mx2(v_[2], v_[3])) * SM_C;           \
    if ((J) > 0) mn_ = mx2(mn_, mrun[NT]);                                                                                  \
    PIN(mn_);                                                                                                               \
    float p_[8];                                                                                                            \
    _Pragma("unroll") for (int j_ = 0; j_ < 8; ++j_)                                                                        \
      p_[j_] = __builtin_amdgcn_exp2f(fmaf(sc[(J) & 1][j_ >> 2][NT][j_ & 3], SM_C, -mn_));                                  \
    const v4u pf_ = {pk(p_[0], p_[1]), pk(p_[2], p_[3]), pk(p_[4], p_[5]), pk(p_[6], p_[7])};                               \
    *(LAS v4u*)(size_t)(aPw + (J) * 16384 + (NT) * 1024) = pf_;                                                             \
    ls[NT] = mfma(ones, pf_, f32x4{0.f, 0.f, 0.f, 0.f});                                                                    \
    if ((J) > 0) al[NT] = __builtin_amdgcn_exp2f(mrun[NT] - mn_);                                                           \
    mrun[NT] = mn_;                                                                                                         \
  }
#define SM_L(J)                                                                                                             \
  _Pragma("unroll") for (int n_ = 0; n_ < 2; ++n_) { lrun[n_] = (J) > 0 ? fmaf(lrun[n_], al[n_], ls[n_][0]) : ls[n_][0]; PIN(lrun[n_]); } \
  if ((J) > 0) { alpha[J] = ntw ? al[1] : al[0]; PIN(alpha[J]); }                                                           \
  if ((J) == 3) { *(LAS float*)(size_t)aRw = (g & 1) ? lrun[1] : lrun[0]; mown = ntw ? mrun[1] : mrun[0]; PIN(mown); }
#define L_READ()                                                                                                            \
  {                                                                                                                         \
    const f32x4 u_ = __builtin_bit_cast(f32x4, lds128(aRr + ntw * 512)), v_ = __builtin_bit_cast(f32x4, lds128(aRr + ntw * 512 + 16)); \
    lown = ((u_[0] + u_[1]) + (u_[2] + u_[3])) + ((v_[0] + v_[1]) + (v_[2] + v_[3]));                                       \
    PIN(lown);                                                                                                              \
  }
#define MIX(NM, NV) _Pragma("unroll") for (int i_ = 0; i_ < (NM); ++i_) { __builtin_amdgcn_sched_group_barrier(0x8, 1, 0); __builtin_amdgcn_sched_group_barrier(0x402, NV, 0); }
#define O_RESCALE(A, J) A *= alpha[J]
#define RD_V(N, S)                                                                                                          \
  const v4u v0_##N = tr_frag(aN + (S), aN + (S) + 2048), p0_##N = lds128(aPr + ((N) & 15) * 4096),                          \
            v1_##N = tr_frag(aN + (S) + 4096, aN + (S) + 6144), p1_##N = lds128(aPr + ((N) & 15) * 4096 + 2048)
#define MM_V(A, N) A = mfma(v0_##N, p0_##N, A); A = mfma(v1_##N, p1_##N, A); PIN(A)
#define CONS_O(S, J) wf[J] = tr_frag(aO[(S) >> 16] + ((S) & 65535), aO[(S) >> 16] + ((S) & 65535) + 4096)
#define PUBLISH_X()                                                                                                         \
  {                                                                                                                         \
    const v4u x_ = pair_rows(enc_fx(ox, (g & 1) ? tB : tA));                                                                \
    stg((g & 1) ? v4u{__float_as_uint(mown), tag32, __float_as_uint(lown), ~tag32} : x_, rsWs,                              \
        !(g & 1) ? WS_X + me * 4096 + w * 512 + ((g >> 1) * 16 + c) * 16                                                   \
                 : (g == 1 && (c >> 2) == ds) ? WS_S + me * 512 + (ntw * 16 + c) * 16 : OOB);                      \
  }
#define XG_BAD() any_bad(((o_.y ^ ((g & 1) ? tB : tA)) & SPARE) | (m_.y ^ tag32) | (m_.w ^ ~tag32))
#define CONS_XG(S)                                                                                                          \
  {                                                                                                                         \
    const unsigned po_ = ((g >> 1) * 16 + c) * 16 + (g & 1) * 8;                                                            \
    v2u o_ = *(const LAS v2u*)(size_t)(ldsw + (S) + po_);                                                                   \
    v4u m_ = lds128(ldsw + (S) + 512 + c * 16);                                                                             \
    if (__builtin_expect(XG_BAD(), 0)) {                                                                                    \
      _Pragma("unroll 1") for (unsigned sp_ = 0; sp_ < SPIN_CAP; ++sp_) {                                                   \
        __builtin_amdgcn_s_sleep(8);                                                                                        \
        MEMFENCE();                                                                                                         \
        const v4u y_ = ldg(rsWs, WS_X + (me ^ 1) * 4096 + w * 512 + (po_ & ~8u));                                          \
        o_ = (g & 1) ? v2u{y_.z, y_.w} : v2u{y_.x, y_.y};                                                                   \
        m_ = ldg(rsWs, WS_S + (me ^ 1) * 512 + (ntw * 16 + c) * 16);                                               \
                                                                                                                  \
        if (!XG_BAD()) break;                                                                                               \
      }                                                                                                                     \
    }                                                                                                                       \
    const float mo_ = __uint_as_float(m_.x), lo_ = __uint_as_float(m_.z), mm_ = mx2(mown, mo_);                           \
    const float e0_ = __builtin_amdgcn_exp2f(mown - mm_), e1_ = __builtin_amdgcn_exp2f(mo_ - mm_);                          \
    const float a_ = lown * e0_, b_ = lo_ * e1_;                                                                            \
    const float inv_ = 1.0f / (a_ + b_);                                                                                    \
    fown = e0_ * inv_;                                                                                                      \
    obase = dec_fx(o_) * (e1_ * inv_);                                                                                        \
    PIN(fown); PIN(obase);                                                                                                  \
  }
#define CHK_OFF(I) (WS_F + ((I) * 64 + lane) * 16)
#define CHK_BAD(U) (((U).y ^ tag32) | ((U).w ^ ~tag32))
#define CHK_ISSUE()                                                                                                         \
  if (blk == KEEPER && w == 7) {                                                                                                 \
    const unsigned o0_ = CHK_OFF(0), o1_ = CHK_OFF(1), o2_ = CHK_OFF(2), o3_ = CHK_OFF(3);                                  \
    DMA_AT("buffer_load_dwordx4", "sc1", rsWs, o0_, 0, lds0, LDS_CHK);                                                      \
    DMA_AT("buffer_load_dwordx4", "sc1", rsWs, o1_, 0, lds0, LDS_CHK + 1024);                                               \
    DMA_AT("buffer_load_dwordx4", "sc1", rsWs, o2_, 0, lds0, LDS_CHK + 2048);                                               \
    DMA_AT("buffer_load_dwordx4", "sc1", rsWs, o3_, 0, lds0, LDS_CHK + 3072);                                               \
  }
#define CHK_DONE()                                                                                                          \
  if (blk == KEEPER && w == 7) {                                                                                                 \
    unsigned d_ = 0;                                                                                                        \
    _Pragma("unroll") for (int i_ = 0; i_ < 4; ++i_) { const v4u u_ = lds128(aL + LDS_CHK + i_ * 1024); d_ |= CHK_BAD(u_); } \
    owed = any_bad(d_);                                                                                                     \
    if (!owed) __builtin_amdgcn_raw_buffer_store_b32(tag32, rsWs, lane == 0 ? 0u : OOB, 0, SC1);                            \
  }
#define PUBLISH_O() stg(pair_rows(enc_bf(oa * fown + obase, tP)), rsWs, offOp)
#define SENTINEL() if (DELAY1 > 0) __builtin_amdgcn_s_sleep(DELAY1); DMA_AT("buffer_load_dword", "sc1", rsWs, offOs, 0, ldsq, LDS_MX)
#define O_GATHER(OFF) v4u og[4]; _Pragma("unroll") for (int i_ = 0; i_ < 4; ++i_) og[i_] = ldg(rsWs, (OFF) + i_ * 1024)
#define O_IMAGE(D)                                                                                                          \
  _Pragma("unroll") for (int i_ = 0; i_ < 4; ++i_) {                                                                        \
    *(LAS v4u*)(size_t)aOw[i_] = dec_bf2(og[i_]);                                                                           \
    D = xor_or(og[i_].y, tA, xor_or(og[i_].w, tB, D));                                                                      \
  }
#define O_PREP() aO[0] = lds0 + rT * 256 + (w ^ (rT & 7)) * 32 + (c & 3) * 8; aO[1] = aO[0] + 65536; aO[2] = aO[0] + 131072; PIN(aO[0]); PIN(aO[1]); PIN(aO[2])
#define TAIL_PREP()                                                                                                         \
  offOp = !(g & 1) ? WS_O + ((kva * 2 + ho) * 32 + (b ^ ((kva * 2 + ho) * 4))) * 4096 + w * 512 + ((g >> 1) * 16 + c) * 16 : OOB;                     \
  offOg = WS_O + (xcd * 32 + ((8 * mg + w) ^ (xcd * 4))) * 4096 + lane * 16;                                                     \
  jP = (xcd < 4 ? 4 : 0) + (c >> 2);                                                                                        \
  offPp = (WS_P + (mg * 8 + nt128) * 65536 + (jP ^ nt128) * 8192 + w * 1024 + (xcd ^ jP) * 128 + (g >> 1) * 64 + (c & 3) * 16) | ((g & 1) << 31); \
  offPq = (c >> 2) == (xcd & 3) ? OOB : offPp ^ (32768 + 512);                                                              \
  offPg = lane < 8 ? OOB : WS_P + (mg * 8 + nt128) * 65536 + (xcd ^ nt128) * 8192 + w * 1024 + lane * 16;                   \
  offOut = (lane & 8) ? OOB : (32 * mg + 4 * xcd + (c & 3)) * 2048 + nt128 * 256 + w * 32 + ((c >> 2) & 1) * 16 + g * 4;    \
  uA = low_nibbles((c & 1) ? tg.z : tg.x); uB = low_nibbles((c & 1) ? tg.w : tg.y); uP = (g & 1) ? uB : uA;                 \
  aF0 = aP + (xcd < 4 ? 1024 : 0); aF1 = aF0 ^ 1024; srcL = (((c >> 2) & 1) * 32 + (xcd & 3) * 4 + (c & 3)) * 4;            \
  PIN(uA); PIN(uB); PIN(uP); PIN(aF0); PIN(aF1); PIN(srcL);                                                                 \
  aOw[0] = lds0 + LDS_P + ((2 * (c >> 2)) ^ ordO) * 2048 + ntw * 1024 + (g * 16 + ds * 4 + (c & 3)) * 16;                                     \
  offOs = lane < 2 ? offOg + 3588 - lane * 8 : OOB; tP = (g & 1) ? tB : tA; PIN(tP);                                                                                 \
  PIN(offOp); PIN(offOg); PIN(offOs); PIN(offPp); PIN(offPq); PIN(offPg); PIN(offOut);                              \
  aOw[1] = aOw[0] ^ 2048; aOw[2] = aOw[0] ^ 16384; aOw[3] = aOw[0] ^ 18432; aSn = ldsq + LDS_MX + lane * 4; tS = spread_byte(lane ? tg.y : tg.x); PIN(tS);                                                                                   \
  PIN(aOw[0]); PIN(aOw[1]); PIN(aOw[2]); PIN(aOw[3]); PIN(aSn)

__global__ void __launch_bounds__(512, 2)
gqa_block_fused(const void* xp, const unsigned char* kp, const unsigned char* vp, const void* wqp, const unsigned char* wop, void* outp,
                void* wsp, void* epochp) {
  __shared__ __attribute__((aligned(1024))) unsigned char smem[LDS_BYTES];
  unsigned long long t64 = __builtin_amdgcn_s_memrealtime();
  FENCE();
  const unsigned lane = threadIdx.x & 63, w = __builtin_amdgcn_readfirstlane(threadIdx.x >> 6);
  const unsigned c = lane & 15, g = lane >> 4, ds = w & 3, ntw = w >> 2;
  const unsigned blk = blockIdx.x, xcd = blk & 7, r = blk >> 3, me = xcd * 32 + r;
  const unsigned b = me >> 3, kva = (me >> 1) & 3;
  const unsigned ho = __builtin_popcount(me & HM) & 1;
  const unsigned kvh = xcd >> 1, dh = xcd & 1, mg = r >> 3;
  const bool burst = (r & 3) == 3;
  const unsigned rot = (xcd + r + ((burst || AROT) ? 2 * mg : 0)) & 7, rotX = rot * 256, rotW = rot * 128 * 8192;
  const unsigned lds0 = (unsigned)(size_t)(LAS unsigned char*)smem, ldsw = lds0 + w * 1024, ldsq = lds0 + w * 256;
  const unsigned region = me * 262144;

  const unsigned rowW = 4 * w + g, rowN = 8 * w + (lane >> 3), chN = (lane & 7) ^ (((rowN >> 1) & 3) << 1);
  unsigned voffK = key_of(rowW) * 256 + (c ^ (rowW & 15)) * 16;
  unsigned voffX = (32 * mg + rowW) * 2048 + (c ^ (rowW & 15)) * 16;
  const unsigned srcN = (rowN & 32) * (WALT ? 2 : 1) + 8 * ((rowN >> 2) & 3) + 4 * ((rowN >> 4) & 1) + (rowN & 3);
  unsigned voffWq = srcN * 8192 + xcd * 1024 + chN * 16;
  PIN(voffK); PIN(voffX); PIN(voffWq);
  asm volatile("" :: "s"(rotX), "s"(rotW), "s"(ldsw), "s"(region));
  FENCE();
  asm volatile("" : "+s"(xp), "+s"(kp), "+s"(vp), "+s"(wqp), "+s"(wop), "+s"(outp), "+s"(wsp), "+s"(epochp), "+s"(t64));
  const unsigned tstart = (unsigned)t64;
  unsigned tbase = tstart, tfirst;
  const unsigned col = (r - (unsigned)((size_t)wqp >> 7)) & 7, nt128 = (r + 2 - (unsigned)((size_t)wop >> 8)) & 7;
  const unsigned ordO = 4 * mg + (nt128 & 3), xorO = (ordO >> 1) * 262144 + (ordO & 1) * 65536;
  const rsrc_t rsX = __builtin_amdgcn_make_buffer_rsrc((void*)xp, 0, 128 * 1024 * 2, 0x00020000);
  const rsrc_t rsWq = __builtin_amdgcn_make_buffer_rsrc((void*)wqp, 0, 1024 * 4096 * 2, 0x00020000);
  const rsrc_t rsK = __builtin_amdgcn_make_buffer_rsrc((void*)(kp + region), 0, 262144, 0x00020000);
  const rsrc_t rsV = __builtin_amdgcn_make_buffer_rsrc((void*)(vp + region), 0, 262144, 0x00020000);
  const rsrc_t rsWo = __builtin_amdgcn_make_buffer_rsrc((void*)(wop + (size_t)kvh * 2097152), 0, 2097152, 0x00020000);
  const rsrc_t rsWs = __builtin_amdgcn_make_buffer_rsrc(wsp, 0, WS_BYTES, 0x00020000);
  const rsrc_t rsOut = __builtin_amdgcn_make_buffer_rsrc(outp, 0, 128 * 1024 * 2, 0x00020000);
  voffWq += col * 128;
  DMA_R("", rsX, voffX, rotX, 0, 2047, 0);  DMA_R("", rsWq, voffWq, rotW, 0, 8388607, 8192);  DMA_R("", rsWq, voffWq, rotW, 524288, 8388607, 16384);  DMA_R("", rsX, voffX, rotX, 256, 2047, 24576);  DMA_R("", rsWq, voffWq, rotW, 1048576, 8388607, 32768);  SMALL_ISSUE();
  FENCE();
  const unsigned srcW = 8 * ((rowW >> 2) & 3) + 4 * (rowW >> 4) + (rowW & 3);
  const unsigned voffWo = (64 * dh + srcW) * 2048 + nt128 * 256 + (c ^ ((rowW & 7) << 1)) * 16;
  const unsigned voffV2 = key_of(rowN) * 256 + chN * 16 + ho * 128, voffV1 = voffV2 ^ 128;
  const unsigned voffQ = WS_Q + b * 32768 + ((kva * 2 + ntw) ^ (b & 7)) * 4096 + (c >> 2) * 1024 + ds * 256 + g * 64 + (c & 3) * 16;
  const unsigned offQd = WS_Q + (8 * mg + 4 * ntw + (c >> 2)) * 32768 + (xcd ^ (4 * ntw + (c >> 2))) * 4096 + col * 512 + ds * 128 +
                         (g >> 1) * 64 + (c & 3) * 16;
  const unsigned offQp = sel(threadIdx.x == 16, WS_F + me * 16, offQd | ((g & 1) << 31));
  const unsigned voffXg = sel(g < 2, WS_X + (me ^ 1) * 4096 + w * 512 + lane * 16, (WS_S + (me ^ 1) * 512 + (ntw * 16 + c) * 16) | ((g & 1) << 31));

  unsigned aK[4], aX[4];
#pragma unroll
  for (int s = 0; s < 4; ++s) { aK[s] = lds0 + c * 256 + ((4 * s + g) ^ c) * 16; aX[s] = aK[s] + ntw * 4096; PIN(aX[s]); }
  const unsigned rT = 4 * g + (c >> 2);
  const unsigned aN = lds0 + rT * 128 + (ds ^ ((rT >> 1) & 3)) * 32 + (c & 3) * 8;
  const unsigned aL = lds0 + lane * 16;
  unsigned aP = aL + LDS_P;
  PIN(aP);
  const unsigned aLw = aL + w * 1024, aPw = aP + w * 2048, aPr = aP + ntw * 1024;
  unsigned aRw = lds0 + LDS_MX + ((g & 1) * 16 + c) * 32 + w * 4, aRr = lds0 + LDS_MX + c * 32;
  PIN(aRw); PIN(aRr);

  v4u tg;
  bool owed = false;
  unsigned tag32, tA, tB, tP, tS, offOs, offOp, offOg, offPp, offPq, offPg, offOut, aOw[4], aSn, aO[3], aF0, aF1, srcL, jP, uA, uB, uP;
  v4u kf[4][8], qf[2][4], wf[16];
  f32x4 qa = {0.f, 0.f, 0.f, 0.f}, oa = {0.f, 0.f, 0.f, 0.f}, ox = {0.f, 0.f, 0.f, 0.f}, ls[2], sc[2][2][2], obase;
  const v4u ones = {0x3F803F80u, 0x3F803F80u, 0x3F803F80u, 0x3F803F80u};
  float mrun[2], lrun[2], al[2], alpha[4], mown, lown, fown;

  if (burst) {
    SYNC(5); RD_X(0, 0); DMA_R("", rsWq, voffWq, rotW, 1572864, 8388607, 40960); FENCE(); FENCE();
    SYNC(5); RD_W(0, 8192); DMA("nt", rsK, voffK, 0, 49152); FENCE(); FENCE();
    SYNC(5); RD_W(1, 16384); DMA("nt", rsK, voffK, 1024, 57344); FENCE(); MM_W(0, x0_0, x1_0); FENCE();
    SYNC(5); RD_X(1, 24576); DMA_R("", rsX, voffX, rotX, 512, 2047, 0); FENCE(); MM_W(1, x2_0, x3_0); FENCE();
    SYNC(5); RD_W(2, 32768); DMA_R("", rsWq, voffWq, rotW, 2097152, 8388607, 8192); FENCE(); T_MARK(); FENCE();
    SYNC(4); RD_W(3, 40960); DMA_R("", rsWq, voffWq, rotW, 2621440, 8388607, 16384); FENCE(); MM_W(2, x0_1, x1_1); READ_EPOCH(); FENCE();
    SYNC(4); DMA_R("", rsX, voffX, rotX, 768, 2047, 24576); FENCE(); MM_W(3, x2_1, x3_1); K_INIT(0); CONS_K(49152, 0, 0); FENCE();
    SYNC(4); DMA_R("", rsWq, voffWq, rotW, 3145728, 8388607, 32768); FENCE(); CONS_K(57344, 0, 1); FENCE();
    SYNC(4); RD_X(2, 0); DMA_R("", rsWq, voffWq, rotW, 3670016, 8388607, 40960); FENCE(); FENCE();
    SYNC(4); RD_W(4, 8192); DMA_R("", rsX, voffX, rotX, 1024, 2047, 49152); FENCE(); FENCE();
    SYNC(4); RD_W(5, 16384); DMA_R("", rsWq, voffWq, rotW, 4194304, 8388607, 57344); FENCE(); MM_W(4, x0_2, x1_2); FENCE();
    SYNC(4); RD_X(3, 24576); DMA_R("", rsWq, voffWq, rotW, 4718592, 8388607, 0); FENCE(); MM_W(5, x2_2, x3_2); FENCE();
    SYNC(4); RD_W(6, 32768); DMA_R("", rsX, voffX, rotX, 1280, 2047, 8192); FENCE(); FENCE();
    SYNC(4); RD_W(7, 40960); DMA_R("", rsWq, voffWq, rotW, 5242880, 8388607, 16384); FENCE(); MM_W(6, x0_3, x1_3); FENCE();
    SYNC(4); RD_X(4, 49152); DMA_R("", rsWq, voffWq, rotW, 5767168, 8388607, 24576); FENCE(); MM_W(7, x2_3, x3_3); FENCE();
    SYNC(4); RD_W(8, 57344); DMA_R("", rsX, voffX, rotX, 1536, 2047, 32768); FENCE(); FENCE();
    SYNC(4); RD_W(9, 0); DMA_R("", rsWq, voffWq, rotW, 6291456, 8388607, 40960); FENCE(); MM_W(8, x0_4, x1_4); FENCE();
    SYNC(4); RD_X(5, 8192); DMA_R("", rsWq, voffWq, rotW, 6815744, 8388607, 49152); FENCE(); MM_W(9, x2_4, x3_4); FENCE();
    SYNC(4); RD_W(10, 16384); DMA_R("", rsX, voffX, rotX, 1792, 2047, 57344); FENCE(); FENCE();
    SYNC(4); RD_W(11, 24576); DMA_R("", rsWq, voffWq, rotW, 7340032, 8388607, 0); FENCE(); MM_W(10, x0_5, x1_5); FENCE();
    SYNC(4); RD_X(6, 32768); DMA_R("", rsWq, voffWq, rotW, 7864320, 8388607, 8192); FENCE(); MM_W(11, x2_5, x3_5); FENCE();
    SYNC(4); RD_W(12, 40960); DMA("nt", rsK, voffK, 8192, 16384); FENCE(); FENCE();
    SYNC(4); RD_W(13, 49152); DMA("nt", rsK, voffK, 9216, 24576); FENCE(); MM_W(12, x0_6, x1_6); FENCE();
    SYNC(4); RD_X(7, 57344); DMA("nt", rsK, voffK, 131072, 32768); FENCE(); MM_W(13, x2_6, x3_6); FENCE();
    SYNC(4); RD_W(14, 0); DMA("nt", rsK, voffK, 132096, 40960); FENCE(); FENCE();
    SYNC(4); RD_W(15, 8192); DMA("nt", rsK, voffK, 139264, 49152); FENCE(); MM_W(14, x0_7, x1_7); T_PUBLISH(); MM_W(15, x2_7, x3_7); FENCE();
    SYNC(4); DMA("nt", rsK, voffK, 140288, 57344); FENCE(); CONS_K(16384, 0, 2); FENCE();
    SYNC(4); DMA("nt", rsK, voffK, 32768, 0); FENCE(); CONS_K(24576, 0, 3); FENCE();
    SYNC(4); DMA("nt", rsK, voffK, 33792, 8192); FENCE(); CONS_K(32768, 0, 4); FENCE();
    SYNC(4); DMA("nt", rsK, voffK, 40960, 16384); FENCE(); CONS_K(40960, 0, 5); FENCE();
    SYNC(4); DMA("nt", rsK, voffK, 41984, 24576); FENCE(); CONS_K(49152, 0, 6); FENCE();
    SYNC(4); DMA("nt", rsK, voffK, 163840, 32768); FENCE(); CONS_K(57344, 0, 7); FENCE();
    SYNC(4); DMA("nt", rsK, voffK, 164864, 40960); FENCE(); K_INIT(1); CONS_K(0, 1, 0); FENCE();
    SYNC(4); DMA("nt", rsK, voffK, 172032, 49152); FENCE(); CONS_K(8192, 1, 1); FENCE();
    SYNC(4); DMA("nt", rsK, voffK, 173056, 57344); FENCE(); CONS_K(16384, 1, 2); FENCE();
    SYNC(4); DMA("nt", rsK, voffK, 256, 0); FENCE(); CONS_K(24576, 1, 3); FENCE();
    SYNC(4); DMA("nt", rsK, voffK, 1280, 8192); FENCE(); CONS_K(32768, 1, 4); FENCE();
    SYNC(4); DMA("nt", rsK, voffK, 8448, 16384); FENCE(); CONS_K(40960, 1, 5); FENCE();
    SYNC(4); DMA("nt", rsK, voffK, 9472, 24576); FENCE(); CONS_K(49152, 1, 6); FENCE();
    SYNC(4); DMA("nt", rsK, voffK, 131328, 32768); FENCE(); CONS_K(57344, 1, 7); FENCE();
    SYNC(4); DMA("nt", rsK, voffK, 132352, 40960); FENCE(); K_INIT(2); CONS_K(0, 2, 0); FENCE();
    SYNC(4); DMA("nt", rsK, voffK, 139520, 49152); FENCE(); CONS_K(8192, 2, 1); FENCE();
    SYNC(4); T_ISSUE(); DMA("nt", rsK, voffK, 140544, 57344); FENCE(); CONS_K(16384, 2, 2); FENCE();
    SYNC(5); DMA("nt", rsK, voffK, 33024, 0); FENCE(); CONS_K(24576, 2, 3); FENCE();
    SYNC(5); DMA("nt", rsK, voffK, 34048, 8192); FENCE(); CONS_K(32768, 2, 4); FENCE();
    SYNC(5); DMA("nt", rsK, voffK, 41216, 16384); FENCE(); CONS_K(40960, 2, 5); FENCE();
    SYNC(5); DMA("nt", rsK, voffK, 42240, 24576); FENCE(); CONS_K(49152, 2, 6); FENCE();
    SYNC(4); DMA("nt", rsK, voffK, 164096, 32768); FENCE(); CONS_K(57344, 2, 7); T_READ(); FENCE();
    SYNC(4); DMA("nt", rsK, voffK, 165120, 40960); FENCE(); K_INIT(3); CONS_K(0, 3, 0); FENCE();
    SYNC(4); DMA("nt", rsK, voffK, 172288, 49152); FENCE(); CONS_K(8192, 3, 1); FENCE();
    SYNC(4); DMA("nt", rsK, voffK, 173312, 57344); FENCE(); CONS_K(16384, 3, 2); FENCE();
    SYNC(4); DMA("nt", rsV, voffV1, 0, 0); FENCE(); CONS_K(24576, 3, 3); PUBLISH_Q(); FENCE();
  } else {
    SYNC(5); RD_X(0, 0); DMA_R("", rsWq, voffWq, rotW, 1572864, 8388607, 40960); FENCE(); FENCE();
    SYNC_P(5, 9); RD_W(0, 8192); DMA("nt", rsK, voffK, 0, 49152); FENCE(); FENCE();
    SYNC_P(5, 39); RD_W(1, 16384); DMA("nt", rsK, voffK, 1024, 57344); FENCE(); MM_W(0, x0_0, x1_0); FENCE();
    SYNC_P(5, 70); RD_X(1, 24576); DMA("nt", rsK, voffK, 8192, 0); FENCE(); MM_W(1, x2_0, x3_0); FENCE();
    SYNC_P(5, 100); RD_W(2, 32768); DMA("nt", rsK, voffK, 9216, 8192); FENCE(); FENCE();
    SYNC_P(4, 130); RD_W(3, 40960); DMA("nt", rsK, voffK, 131072, 16384); FENCE(); MM_W(2, x0_1, x1_1); READ_EPOCH(); FENCE();
    SYNC_P(4, 160); DMA("nt", rsK, voffK, 132096, 24576); FENCE(); MM_W(3, x2_1, x3_1); K_INIT(0); CONS_K(49152, 0, 0); FENCE();
    SYNC_P(4, 190); DMA("nt", rsK, voffK, 139264, 32768); FENCE(); CONS_K(57344, 0, 1); FENCE();
    SYNC(4); DMA_R("", rsX, voffX, rotX, 512, 2047, 40960); FENCE(); CONS_K(0, 0, 2); FENCE();
    SYNC_P(4, 224); DMA("nt", rsK, voffK, 140288, 49152); FENCE(); CONS_K(8192, 0, 3); FENCE();
    SYNC(4); DMA_R("", rsWq, voffWq, rotW, 2097152, 8388607, 57344); FENCE(); CONS_K(16384, 0, 4); FENCE();
    SYNC_P(4, 263); DMA("nt", rsK, voffK, 32768, 0); FENCE(); CONS_K(24576, 0, 5); FENCE();
    SYNC(4); DMA_R("", rsWq, voffWq, rotW, 2621440, 8388607, 8192); FENCE(); CONS_K(32768, 0, 6); FENCE();
    SYNC_P(4, 303); RD_X(2, 40960); DMA("nt", rsK, voffK, 33792, 16384); FENCE(); FENCE();
    SYNC(4); DMA_R("", rsX, voffX, rotX, 768, 2047, 24576); FENCE(); CONS_K(49152, 0, 7); FENCE();
    SYNC_P(4, 337); RD_W(4, 57344); DMA("nt", rsK, voffK, 40960, 32768); FENCE(); FENCE();
    SYNC(4); DMA_R("", rsWq, voffWq, rotW, 3145728, 8388607, 40960); FENCE(); MM_W(4, x0_2, x1_2); K_INIT(1); CONS_K(0, 1, 0); FENCE();
    SYNC_P(4, 376); RD_W(5, 8192); DMA("nt", rsK, voffK, 41984, 49152); FENCE(); FENCE();
    SYNC(4); DMA_R("", rsWq, voffWq, rotW, 3670016, 8388607, 57344); FENCE(); MM_W(5, x2_2, x3_2); CONS_K(16384, 1, 1); FENCE();
    SYNC_P(4, 416); RD_X(3, 24576); DMA("nt", rsK, voffK, 163840, 0); FENCE(); FENCE();
    SYNC(4); DMA_R("", rsX, voffX, rotX, 1024, 2047, 8192); FENCE(); CONS_K(32768, 1, 2); FENCE();
    SYNC_P(4, 450); RD_W(6, 40960); DMA("nt", rsK, voffK, 164864, 16384); FENCE(); FENCE();
    SYNC(4); DMA_R("", rsWq, voffWq, rotW, 4194304, 8388607, 24576); FENCE(); MM_W(6, x0_3, x1_3); CONS_K(49152, 1, 3); FENCE();
    SYNC_P(4, 489); RD_W(7, 57344); DMA("nt", rsK, voffK, 172032, 32768); FENCE(); FENCE();
    SYNC(4); DMA_R("", rsWq, voffWq, rotW, 4718592, 8388607, 40960); FENCE(); MM_W(7, x2_3, x3_3); CONS_K(0, 1, 4); FENCE();
    SYNC_P(4, 529); RD_X(4, 8192); DMA("nt", rsK, voffK, 173056, 49152); FENCE(); FENCE();
    SYNC(4); DMA_R("", rsX, voffX, rotX, 1280, 2047, 57344); FENCE(); CONS_K(16384, 1, 5); FENCE();
    SYNC_P(4, 563); RD_W(8, 24576); DMA("nt", rsK, voffK, 256, 0); FENCE(); FENCE();
    SYNC(4); DMA_R("", rsWq, voffWq, rotW, 5242880, 8388607, 8192); FENCE(); MM_W(8, x0_4, x1_4); CONS_K(32768, 1, 6); FENCE();
    SYNC_P(4, 602); RD_W(9, 40960); DMA("nt", rsK, voffK, 1280, 16384); FENCE(); FENCE();
    SYNC(4); DMA_R("", rsWq, voffWq, rotW, 5767168, 8388607, 24576); FENCE(); MM_W(9, x2_4, x3_4); CONS_K(49152, 1, 7); FENCE();
    SYNC_P(4, 642); RD_X(5, 57344); DMA("nt", rsK, voffK, 8448, 32768); FENCE(); FENCE();
    SYNC(4); DMA_R("", rsX, voffX, rotX, 1536, 2047, 40960); FENCE(); K_INIT(2); CONS_K(0, 2, 0); FENCE();
    SYNC_P(4, 675); RD_W(10, 8192); DMA("nt", rsK, voffK, 9472, 49152); FENCE(); FENCE();
    SYNC(4); DMA_R("", rsWq, voffWq, rotW, 6291456, 8388607, 57344); FENCE(); MM_W(10, x0_5, x1_5); CONS_K(16384, 2, 1); FENCE();
    SYNC_P(4, 715); RD_W(11, 24576); DMA("nt", rsK, voffK, 131328, 0); FENCE(); FENCE();
    SYNC(4); T_ISSUE(); DMA_R("", rsWq, voffWq, rotW, 6815744, 8388607, 8192); FENCE(); MM_W(11, x2_5, x3_5); CONS_K(32768, 2, 2); FENCE();
    SYNC_P(5, 755); RD_X(6, 40960); DMA("nt", rsK, voffK, 132352, 16384); FENCE(); FENCE();
    SYNC(5); DMA_R("", rsX, voffX, rotX, 1792, 2047, 24576); FENCE(); CONS_K(49152, 2, 3); FENCE();
    SYNC_P(5, 784); RD_W(12, 57344); DMA("nt", rsK, voffK, 139520, 32768); FENCE(); FENCE();
    SYNC(5); DMA_R("", rsWq, voffWq, rotW, 7340032, 8388607, 40960); FENCE(); MM_W(12, x0_6, x1_6); CONS_K(0, 2, 4); FENCE();
    SYNC_P(4, 818); RD_W(13, 8192); DMA("nt", rsK, voffK, 140544, 49152); FENCE(); T_READ(); FENCE();
    SYNC(4); DMA_R("", rsWq, voffWq, rotW, 7864320, 8388607, 57344); FENCE(); MM_W(13, x2_6, x3_6); CONS_K(16384, 2, 5); FENCE();
    SYNC_P(4, 852); RD_X(7, 24576); DMA("nt", rsK, voffK, 33024, 0); FENCE(); FENCE();
    SYNC_P(4, 878); DMA("nt", rsK, voffK, 34048, 8192); FENCE(); CONS_K(32768, 2, 6); FENCE();
    SYNC_P(4, 904); RD_W(14, 40960); DMA("nt", rsK, voffK, 41216, 16384); FENCE(); FENCE();
    SYNC_P(3, 930); RD_W(15, 57344); DMA("nt", rsK, voffK, 42240, 24576); FENCE(); MM_W(14, x0_7, x1_7); CONS_K(49152, 2, 7); MM_W(15, x2_7, x3_7); PUBLISH_Q(); FENCE();
    SYNC_P(5, 962); DMA("nt", rsK, voffK, 164096, 32768); FENCE(); FENCE();
    SYNC_P(5, 994); DMA("nt", rsK, voffK, 165120, 40960); FENCE(); K_INIT(3); CONS_K(0, 3, 0); FENCE();
    SYNC_P(5, 1026); DMA("nt", rsK, voffK, 172288, 49152); FENCE(); CONS_K(8192, 3, 1); FENCE();
    SYNC_P(5, 1058); DMA("nt", rsK, voffK, 173312, 57344); FENCE(); CONS_K(16384, 3, 2); FENCE();
    SYNC_P(5, 1084); DMA("nt", rsV, voffV1, 0, 0); FENCE(); CONS_K(24576, 3, 3); FENCE();
  }
  SYNC_P(4, 1111); DMA("sc1", rsWs, voffQ, 0, 8192); FENCE(); CONS_K(32768, 3, 4); FENCE();
  SYNC_P(4, 1120); DMA("nt", rsV, voffV1, 8192, 16384); FENCE(); CONS_K(40960, 3, 5); FENCE();
  SYNC_P(4, 1147); DMA("nt", rsV, voffV1, 131072, 24576); FENCE(); CONS_K(49152, 3, 6); FENCE();
  SYNC_P(4, 1174); DMA("nt", rsV, voffV1, 139264, 32768); FENCE(); CONS_K(57344, 3, 7); FENCE();
  SYNC_P(3, 1201); DMA("nt", rsV, voffV2, 0, 40960); FENCE(); CONS_G(8192); FENCE();
  SYNC_P(4, 1229); Q_READ(); DMA("nt", rsV, voffV2, 8192, 8192); FENCE(); FENCE(); SM_S(0, 0); SM_PIN(0, 0); FENCE(); DMA("nt", rsV, voffV2, 131072, 49152); FENCE(); FENCE(); SM_S(0, 1); SM_PIN(0, 1); SM_M(0); FENCE();
  SYNC_P(4, 1284); RD_M(0); DMA("nt", rsV, voffV2, 139264, 57344); FENCE(); FENCE(); SM_S(1, 0); SM_B(0, 0); SM_PIN(1, 0); MIX(8, 4); FENCE(); DMA("nt", rsV, voffV1, 32768, 65536); FENCE(); FENCE(); SM_S(1, 1); SM_B(0, 1); SM_PIN(1, 1); MIX(8, 4); FENCE(); DMA("nt", rsV, voffV1, 40960, 73728); FENCE(); SM_L(0); SM_M(1); FENCE();
  SYNC_P(7, 1367); RD_V(0, 0); RD_V(1, 16384); RD_V(2, 24576); T_LEAD(); CHK_ISSUE(); DMA("nt", rsV, voffV1, 163840, 81920); FENCE(); MM_V(ox, 0); MM_V(ox, 1); MM_V(ox, 2); FENCE();
  SYNC_P(4, 1395); RD_V(3, 32768); RD_V(16, 40960); RD_V(17, 8192); DMA("nt", rsV, voffV1, 172032, 0); FENCE(); MM_V(ox, 3); MM_V(oa, 16); MM_V(oa, 17); FENCE();
  SYNC_P(4, 1423); RD_V(18, 49152); RD_V(19, 57344); DMA("nt", rsV, voffV2, 32768, 8192); FENCE(); MM_V(oa, 18); MM_V(oa, 19); FENCE();
  SYNC_P(4, 1450); RD_M(1); DMA("nt", rsV, voffV2, 40960, 16384); FENCE(); FENCE(); SM_S(2, 0); SM_B(1, 0); SM_PIN(2, 0); MIX(8, 4); FENCE(); DMA("nt", rsV, voffV2, 163840, 24576); FENCE(); FENCE(); SM_S(2, 1); SM_B(1, 1); SM_PIN(2, 1); MIX(8, 4); FENCE(); DMA("nt", rsV, voffV2, 172032, 32768); FENCE(); SM_L(1); SM_M(2); FENCE();
  SYNC_P(5, 1533); RD_V(4, 65536); RD_V(5, 73728); RD_V(6, 81920); DMA("nt", rsV, voffV1, 256, 40960); FENCE(); CHK_DONE(); O_RESCALE(ox, 1); MM_V(ox, 4); MM_V(ox, 5); MM_V(ox, 6); FENCE();
  SYNC_P(4, 1561); RD_V(7, 0); RD_V(20, 8192); DMA("nt", rsV, voffV1, 8448, 49152); FENCE(); FENCE();
  SYNC_P(4, 1589); RD_V(21, 16384); DMA("nt", rsV, voffV1, 131328, 0); FENCE(); MM_V(ox, 7); O_RESCALE(oa, 1); MM_V(oa, 20); MM_V(oa, 21); FENCE();
  SYNC_P(4, 1617); RD_M(2); RD_V(22, 24576); DMA("nt", rsV, voffV1, 139520, 8192); FENCE(); FENCE(); SM_S(3, 0); SM_B(2, 0); SM_PIN(3, 0); MIX(8, 4); FENCE(); DMA("nt", rsV, voffV2, 256, 16384); FENCE(); FENCE(); SM_S(3, 1); SM_B(2, 1); SM_PIN(3, 1); MIX(8, 4); FENCE(); DMA("nt", rsV, voffV2, 8448, 57344); FENCE(); SM_L(2); SM_M(3); FENCE();
  SYNC_P(4, 1696); RD_V(23, 32768); RD_V(8, 40960); RD_V(9, 49152); DMA("nt", rsV, voffV1, 33024, 24576); FENCE(); MM_V(oa, 22); MM_V(oa, 23); O_RESCALE(ox, 2); MM_V(ox, 8); MM_V(ox, 9); FENCE();
  SYNC_P(4, 1722); RD_V(10, 0); DMA("nt", rsV, voffV1, 41216, 32768); FENCE(); FENCE();
  SYNC_P(4, 1748); RD_V(11, 8192); DMA("nt", rsV, voffV1, 164096, 0); FENCE(); MM_V(ox, 10); MM_V(ox, 11); FENCE();
  SYNC_P(4, 1775); RD_M(3); RD_V(24, 16384); DMA("nt", rsV, voffV1, 172288, 8192); FENCE(); FENCE(); SM_B(3, 0); FENCE(); DMA("nt", rsV, voffV2, 131328, 40960); FENCE(); FENCE(); SM_B(3, 1); SM_L(3); FENCE();
  SYNC_P(4, 1808); RD_V(25, 57344); RD_V(12, 24576); DMA("nt", rsV, voffV2, 139520, 16384); FENCE(); O_RESCALE(oa, 2); MM_V(oa, 24); L_READ(); FENCE();
  SYNC_P(4, 1825); RD_V(13, 32768); DMA("nt", rsV, voffV2, 33024, 24576); FENCE(); MM_V(oa, 25); O_RESCALE(ox, 3); MM_V(ox, 12); FENCE();
  SYNC_P(3, 1870); RD_V(14, 0); RD_V(15, 8192); DMA("nt", rsV, voffV2, 41216, 32768); FENCE(); MM_V(ox, 13); MM_V(ox, 14); MM_V(ox, 15); PUBLISH_X(); FENCE();
  SYNC_P(5, 1915); DMA("nt", rsV, voffV2, 164096, 0); FENCE(); FENCE();
  SYNC_P(5, 1970); RD_V(26, 40960); DMA("nt", rsV, voffV2, 172288, 8192); FENCE(); FENCE();
  SYNC(5); RD_V(27, 16384); O_PREP(); DMA_X("", rsWo, voffWo, xorO, 0, 40960); FENCE(); MM_V(oa, 26); TAIL_PREP(); FENCE();
  SYNC(5); RD_V(28, 24576); DMA("sc1", rsWs, voffXg, 0, 16384); FENCE(); MM_V(oa, 27); FENCE();
  SYNC(5); RD_V(29, 32768); DMA_X("", rsWo, voffWo, xorO, 65536, 24576); FENCE(); O_RESCALE(oa, 3); MM_V(oa, 28); FENCE();
  SYNC(4); RD_V(30, 0); DMA_X("", rsWo, voffWo, xorO, 262144, 32768); FENCE(); MM_V(oa, 29); FENCE();
  SYNC(4); RD_V(31, 8192); FENCE(); MM_V(oa, 30); MM_V(oa, 31); FENCE();


  asm volatile("s_waitcnt vmcnt(2)" ::: "memory"); CONS_XG(16384); PUBLISH_O(); FENCE(); DMA_X("", rsWo, voffWo, xorO, 327680, 0); DMA_X("", rsWo, voffWo, xorO, 524288, 49152); DMA_X("", rsWo, voffWo, xorO, 589824, 57344); DMA_X("", rsWo, voffWo, xorO, 786432, 65536); DMA_X("", rsWo, voffWo, xorO, 851968, 73728); SYNC(6); CONS_O(40960, 0); CONS_O(24576, 1); CONS_O(32768, 2); FENCE(); DMA_X("", rsWo, voffWo, xorO, 1048576, 8192); DMA_X("", rsWo, voffWo, xorO, 1114112, 16384); DMA_X("", rsWo, voffWo, xorO, 1310720, 81920); DMA_X("", rsWo, voffWo, xorO, 1376256, 131072); SYNC(5); CONS_O(0, 3); CONS_O(49152, 4); CONS_O(57344, 5); CONS_O(65536, 6); FENCE(); SENTINEL(); DMA_X("", rsWo, voffWo, xorO, 1572864, 24576); DMA_X("", rsWo, voffWo, xorO, 1638400, 32768); DMA_X("", rsWo, voffWo, xorO, 1835008, 40960); DMA_X("", rsWo, voffWo, xorO, 1900544, 139264); asm volatile("s_waitcnt vmcnt(4)" ::: "memory");

  {
    unsigned y, left, m = 0;
    asm volatile("ds_read_b32 %0, %2\n\ts_mov_b32 %1, %6\n\ts_waitcnt lgkmcnt(0)\n.La%=:\n\t"
                 "v_xor_b32 %0, %0, %3\n\tv_and_b32 %0, %7, %0\n\tv_cmp_ne_u32 vcc, 0, %0\n\ts_and_b32 vcc_lo, vcc_lo, 3\n\t"
                 "s_cbranch_scc0 .Lb%=\n\ts_sub_u32 %1, %1, 1\n\ts_cbranch_scc1 .Lb%=\n\t"
                 "buffer_load_dword %0, %4, %5, 0 offen sc1\n\ts_waitcnt vmcnt(0)\n\ts_branch .La%=\n.Lb%=:"
                 : "=&v"(y), "=&s"(left) : "v"(aSn), "v"(tS), "v"(offOs), "s"(rsWs), "i"(SPIN_CAP), "i"(SPARE) : "memory", "scc", "vcc");
    {
      v4u og[4];
      const unsigned so = offOg;
      asm volatile("buffer_load_dwordx4 %0, %4, %5, 0 offen sc1\n\tbuffer_load_dwordx4 %1, %4, %5, 0 offen offset:1024 sc1\n\t"
                   "buffer_load_dwordx4 %2, %4, %5, 0 offen offset:2048 sc1\n\tbuffer_load_dwordx4 %3, %4, %5, 0 offen offset:3072 sc1"
                   : "=&v"(og[0]), "=&v"(og[1]), "=&v"(og[2]), "=&v"(og[3]) : "v"(so), "s"(rsWs) : "memory");
      FENCE();
      SYNC(4); CONS_O(73728, 7); CONS_O(8192, 8); CONS_O(16384, 9); CONS_O(81920, 10); CONS_O(131072, 11); CONS_O(24576, 12); CONS_O(32768, 13); CONS_O(40960, 14); CONS_O(139264, 15); FENCE();
      FENCE();
      asm volatile("s_waitcnt vmcnt(0)" : "+v"(og[0]), "+v"(og[1]), "+v"(og[2]), "+v"(og[3]) :: "memory");
      O_IMAGE(m);
    }
    if (__builtin_expect(any_bad(m & SPARE), 0)) {
#pragma unroll 1
      for (unsigned sp = 0; sp < SPIN_CAP; ++sp) {
        MEMFENCE();
        m = 0;
        O_GATHER(offOg);
        O_IMAGE(m);
        if (!any_bad(m & SPARE)) break;
      }
    }

  }
  SYNC(0);
  f32x4 pa[2] = {{0.f, 0.f, 0.f, 0.f}, {0.f, 0.f, 0.f, 0.f}};
  {
    v4u of[32];
#define OF_ADDR(F) (((F) < 16 ? aF0 : aF1) + ((F) & 15) * 2048)
#pragma unroll
    for (int f = 0; f < 6; ++f) of[f] = lds128(OF_ADDR(f));

    FENCE();
#pragma unroll
    for (int f = 0; f < MSPLIT; ++f) {
      pa[f >> 4] = mfma(wf[f & 15], of[f], pa[f >> 4]);
      PIN(pa[f >> 4]);
      if (f < 6) { of[6 + 2 * f] = lds128(OF_ADDR(6 + 2 * f)); of[7 + 2 * f] = lds128(OF_ADDR(7 + 2 * f)); }
      else if (f + 12 < 32) of[f + 12] = lds128(OF_ADDR(f + 12));
      if (f == MSPLIT - 1) _Pragma("unroll") for (int e = f + 13; e < 32; ++e) of[e] = lds128(OF_ADDR(e));
      FENCE();
    }
    stg(pair_rows(enc_ps(pa[0], uP)), rsWs, offPp);
#pragma unroll
    for (int f = MSPLIT; f < MSPLIT + MLEN; ++f) pa[1] = mfma(wf[f & 15], of[f], pa[1]);
    PIN(pa[1]);
    MIX(MLEN, MV);
    FENCE();
#pragma unroll
    for (int f = MSPLIT + MLEN; f < 32; ++f) pa[1] = mfma(wf[f & 15], of[f], pa[1]);
  }
  const v4u pl = pair_rows(enc_ps(pa[1], uP));
  stg(pl, rsWs, offPq);
  v4u u;
#pragma unroll
  for (int i = 0; i < 4; ++i) u[i] = __builtin_amdgcn_ds_bpermute(srcL, pl[i]);
  if (DELAY2 < 0) asm volatile("s_waitcnt vmcnt(0)" ::: "memory"); else if (DELAY2 > 0) __builtin_amdgcn_s_sleep(DELAY2 < 0 ? 0 : DELAY2);
  {

    unsigned long long miss = ~0xFFull;
#pragma unroll 1
    for (unsigned sp = 0; sp < SPIN_CAP; ++sp) {
      const bool want = ((miss >> (lane & 56)) & 0xFFull) != 0ull;
      MEMFENCE();
      const v4u y = ldg(rsWs, want ? offPg : OOB);
      u = want ? y : u;
      miss = __builtin_amdgcn_ballot_w64((xor_or(u.y, uA, u.w ^ uB) & LOWN) != 0u);
      if (miss == 0ull) break;
      if (POLL2) __builtin_amdgcn_s_sleep(POLL2);
    }
    const f32x4 lo = dec_ps(u.x, u.y), hi = dec_ps(u.z, u.w);
    float s4[4], s2[2];
#pragma unroll
    for (int i = 0; i < 4; ++i) {
      const auto t = __builtin_amdgcn_permlane32_swap(__float_as_uint(lo[i]), __float_as_uint(hi[i]), false, false);
      s4[i] = __uint_as_float(t[0]) + __uint_as_float(t[1]);
    }
#pragma unroll
    for (int i = 0; i < 2; ++i) {
      const auto t = __builtin_amdgcn_permlane16_swap(__float_as_uint(s4[i]), __float_as_uint(s4[i + 2]), false, false);
      const float a = __uint_as_float(t[0]) + __uint_as_float(t[1]);
      s2[i] = a + __int_as_float(__builtin_amdgcn_mov_dpp(__float_as_int(a), 0x128, 0xF, 0xF, true));
    }
    __builtin_amdgcn_raw_buffer_store_b32(pk(s2[0], s2[1]), rsOut, offOut, 0, 0);
  }
  if (__builtin_expect(owed, 0)) {
#pragma unroll 1
    for (unsigned sp = 0; sp < SPIN_CAP; ++sp) {
      MEMFENCE();
      const v4u u0 = ldg(rsWs, CHK_OFF(0)), u1 = ldg(rsWs, CHK_OFF(1)), u2 = ldg(rsWs, CHK_OFF(2)), u3 = ldg(rsWs, CHK_OFF(3));
      if (!any_bad(CHK_BAD(u0) | CHK_BAD(u1) | CHK_BAD(u2) | CHK_BAD(u3))) break;
      __builtin_amdgcn_s_sleep(8);
    }
    __builtin_amdgcn_raw_buffer_store_b32(tag32, rsWs, lane == 0 ? 0u : OOB, 0, SC1);
  }
  asm volatile(".Lgqa_end:");
}

extern "C" void kernel_launch(void* const* d_in, const int* in_sizes, int n_in, void* d_out, int out_size, void* d_ws, size_t ws_size,
                              hipStream_t stream) {
  gqa_block_fused<<<256, 512, 0, stream>>>(d_in[0], (const unsigned char*)d_in[1], (const unsigned char*)d_in[2], d_in[3],
                                           (const unsigned char*)d_in[4], d_out, d_ws, d_ws);
}
```
